```python
import torch

def _softmax(x, axis=-1):
    return torch.softmax(x, dim=axis)
def _gelu(x):
    return torch.nn.functional.gelu(x, approximate="tanh")
def _silu(x):
    return torch.nn.functional.silu(x)
def _sigmoid(x):
    return torch.sigmoid(x)
def _layernorm(x, axis=-1, eps=1e-5):
    m = x.mean(dim=axis, keepdim=True)
    v = x.var(dim=axis, keepdim=True, unbiased=False)
    return (x - m) / torch.sqrt(v + eps)
def _scatter_add(tgt, idx, src, axis=0):
    return tgt.clone().index_add_(axis, idx, src)
def _segment_sum(src, seg, n):
    out = torch.zeros((n,) + src.shape[1:], dtype=src.dtype)
    return out.index_add_(0, seg, src)
def _pad(x, pw):
    flat = tuple(v for pair in reversed(pw) for v in pair)
    return torch.nn.functional.pad(x, flat)

kernel_name = 'gated_mix__linear_attn_block__dense_activation_gelu__prompt'

def setup_inputs():

    _w = (lambda a: (lambda t: t.to(torch.bfloat16) if t.is_floating_point() else t)(torch.as_tensor(a)))
    def _A_setup():
        B, S, D = 1, 2048, 512
        return {
            "x": _w((lambda *s: torch.randn(*s, dtype=torch.bfloat16) * 0.05)(B, S, D)),
            "wqkv": _w((lambda *s: torch.randn(*s, dtype=torch.bfloat16) * 0.8)(D, 3 * D)),
            "wo": _w((lambda *s: torch.randn(*s, dtype=torch.bfloat16) * 0.05)(D, D)),
        }
    _A_in = _A_setup()

    def _B_setup():
        B, S, D, F = 1, 2048, 512, 1376
        return {
            "x": _w((lambda *s: torch.randn(*s, dtype=torch.bfloat16) * 0.05)(B, S, D)),
            "w1": _w((lambda *s: torch.randn(*s, dtype=torch.bfloat16) * 3.2)(D, F) * (D ** -0.5)),
            "b1": _w((lambda *s: torch.randn(*s, dtype=torch.bfloat16) * 3.2)(F) * 0.01),
            "w2": _w((lambda *s: torch.randn(*s, dtype=torch.bfloat16) * 0.4)(F, D) * (F ** -0.5)),
            "b2": _w((lambda *s: torch.randn(*s, dtype=torch.bfloat16) * 0.8)(D) * 0.01),
        }
    _B_in = _B_setup()

    return {
        "A_x": _A_in["x"],
        "A_wqkv": _A_in["wqkv"],
        "A_wo": _A_in["wo"],
        "B_x": _B_in["x"],
        "B_w1": _B_in["w1"],
        "B_b1": _B_in["b1"],
        "B_w2": _B_in["w2"],
        "B_b2": _B_in["b2"],
        "gate_w": _w((lambda *s: torch.randn(*s, dtype=torch.bfloat16) * 0.05)(512) * 16.0),
    }

def reference(A_x, A_wqkv, A_wo, B_x, B_w1, B_b1, B_w2, B_b2, gate_w):
    def _A_ref(x, wqkv, wo):
        B, S, D = x.shape
        H = 32; Dh = D // H
        qkv = (lambda x, *d: x.permute(*d))((x @ wqkv).reshape(B, S, 3, H, Dh), 2, 0, 3, 1, 4)
        q, k, v = qkv[0], qkv[1], qkv[2]
        phi = lambda t: torch.where(t > 0, t + 1, torch.exp(t)) + 1e-6
        pq, pk = phi(q), phi(k)
        kv = torch.einsum("bhsd,bhse->bhde", pk, v)
        z = torch.einsum("bhsd->bhd", pk)
        num = torch.einsum("bhqd,bhde->bhqe", pq, kv)
        den = torch.einsum("bhqd,bhd->bhq", pq, z)[..., None] + 1e-6
        o = num / den
        return (lambda x, *d: x.permute(*d))(o, 0, 2, 1, 3).reshape(B, S, D) @ wo

    def _B_ref(x, w1, b1, w2, b2):
        h = torch.einsum("bsd,df->bsf", x, w1) + b1
        h = _gelu(h)
        return torch.einsum("bsf,fd->bsd", h, w2) + b2

    x = A_x
    g = _sigmoid((x * gate_w).sum(-1, keepdims=True))
    a = _A_ref(x, A_wqkv, A_wo)
    b = _B_ref(x, B_w1, B_b1, B_w2, B_b2)
    return g * a + (1 - g) * b
```

```cpp
#include <hip/hip_runtime.h>
#include <hip/hip_bf16.h>
#include <stdint.h>
#include <utility>
#include <initializer_list>

namespace {

typedef __attribute__((ext_vector_type(8))) __bf16 bf16x8;
typedef __attribute__((ext_vector_type(4))) __bf16 bf16x4;
typedef __attribute__((ext_vector_type(4))) short s16x4;
typedef __attribute__((ext_vector_type(4))) float f32x4;
typedef __attribute__((ext_vector_type(4))) unsigned u32x4;
typedef __attribute__((ext_vector_type(2))) unsigned u32x2;
typedef __amdgpu_buffer_rsrc_t rsrc_t;
#define LAS __attribute__((address_space(3)))
#define DEV __device__ __forceinline__
template <int V> using IC = std::integral_constant<int, V>;

constexpr int S_ = 2048, D_ = 512, F_ = 1376, N3_ = 1536;
constexpr int NSLOT = 9, SLOT = 16384;
constexpr int L_MISC = NSLOT * SLOT;
constexpr int L_ZP = L_MISC;
constexpr int L_KVA = L_MISC + 1024;
constexpr int L_ZA = L_MISC + 3072;
constexpr int L_B2 = L_MISC + 3328;
constexpr int L_G = L_MISC + 3584;
constexpr int L_POLL = L_MISC + 3840;
constexpr int L_B1 = L_MISC + 4096;
constexpr int L_DUMMY = L_MISC + 12288;
constexpr int LDS_BYTES = L_MISC + 12544;

constexpr unsigned W_EPOCH = 0;
constexpr unsigned FL1 = 8, FSTR = 384;
constexpr unsigned W_FH = FSTR;
constexpr unsigned W_FK = W_FH + 128 * FSTR;
constexpr unsigned W_G = 704 * 1024;
constexpr unsigned W_KV = 720 * 1024;
constexpr unsigned W_ZP = W_KV + 32 * 16 * 512;
constexpr unsigned W_A2 = 1u << 20;
static_assert(W_FK + 32 * FSTR <= W_G && W_ZP + 32768 <= W_A2, "workspace map");
constexpr unsigned W_END = W_A2 + 128 * 60 * 1024;
constexpr unsigned OOB = 0x10000000u;

#define LA 5
#define LA2 2
constexpr int FA_STEP = 134;
constexpr int FB_STEP = 156;
constexpr int FL_STEP = 4;
constexpr int REL_STEP = 159;
constexpr int U1 = 13, U2 = 17, U3 = 20;
#define S01 2
#define LAGP 2
#define NOPRE 0
#define ROTM 8
#define LW 0
#define FBA 156
#define DLYA 12
#define AONE 1
#define SLP 8
#define NOCOOP 1
constexpr int CAPA = 3;
constexpr int DLY_A = DLYA, DLY_F = 2, SLEEP = SLP;

constexpr int NB = 27;
constexpr int T_W = 20, T_A = 35, T_KVS = 50, NT = 51;
enum { OP_PI = 300, OP_PC = 310, OP_GI = 320, OP_GC, OP_MISC, OP_SLEEP, OP_COOP, OP_S2, OP_S1, OP_W = 340, OP_END = -1 };

struct Plan {
  int slot[NT];
  int wait[NB];
  int op[NB + 1][16];
  int arg[NB + 1][16];
  bool ok;
};
constexpr int need_bar(int t) { return t < 16 ? t / 2 : t == 16 ? (NOPRE ? 9 : 8) : t < T_W ? 9 : t < T_A ? 12 + t - T_W : 12 + t - T_A; }
constexpr int rel_bar(int t) {
  if (t < 16) return t / 2 + 2;
  if (t == T_KVS) return 10;
  if (t < T_W) return 128 + 16 * (t - 15) - 1 <= REL_STEP ? 10 : 11;
  return need_bar(t) + 1;
}
constexpr Plan make_plan(bool isa) {
  int src[NB + 1][16] = {};
  for (int b = 0; b <= NB; b++) src[b][0] = OP_END;
  auto set = [&](int b, std::initializer_list<int> l) { int k = 0; for (int v : l) src[b + 1][k++] = v; src[b + 1][k] = OP_END; };
  set(-1, {OP_MISC});
  set(0, {4, 5});
  set(1, {6, 7, 8});
  if (S01 == 3) { set(0, {4, 5, 6}); set(1, {7, 8}); }
  if (S01 == 4) { set(0, {4, 5, 6, 7}); set(1, {8}); }
  if (S01 == 5) { set(0, {4, 5, 6, 7, 8}); set(1, {}); }
  if (S01 == 6) { set(-1, {OP_MISC, 4}); set(0, {5, 6, 7}); set(1, {8}); }
  for (int b = 2; b <= 6; b++) set(b, {2 * b + 5, 2 * b + 6});
  set(7, {19, OP_W + 1, OP_COOP});
  set(8, {OP_W + 2});
  if (LW == 1) { set(7, {19}); set(8, {OP_COOP, OP_W + 2}); }
  if (LW == 2) { set(5, {15}); set(6, {16}); set(7, {17}); set(8, {18, 19, OP_COOP, OP_W + 2}); }
  if (LW == 3) { set(5, {15, 16}); set(6, {17}); set(7, {18}); set(8, {19, OP_COOP, OP_W + 2}); }
  if (LW == 4) { set(5, {15, 16}); set(6, {17}); set(7, {18}); set(8, {OP_COOP, 19, OP_W + 2}); }
  if (NOCOOP == 1 || (NOCOOP == 2 && isa)) set(7, {19, OP_W + 1});
  if (NOCOOP == 2 && !isa) { set(6, {17, 18, OP_COOP}); }
  if (NOCOOP == 3 && isa) { set(5, {15, 16, OP_COOP}); set(7, {19, OP_W + 1}); }
  set(9, {OP_W + 4});
  if (isa) set(10, {OP_SLEEP, OP_PI + 4, OP_PC + 4, OP_GI, OP_PI + 0, OP_PC + 0, AONE ? OP_S1 : OP_S2, OP_GC});
  else set(10, {OP_SLEEP, OP_PI + 0, OP_PC + 0, OP_S2});
  for (int b = 11; b < NB; b++) {
    int k = 0;
    if (b == U1 && !isa) src[b + 1][k++] = OP_PC + 1;
    if (b == U2) src[b + 1][k++] = OP_PC + 2;
    if (b == U3) src[b + 1][k++] = OP_PC + 3;
    if (b == U1 - 1 && !isa) src[b + 1][k++] = OP_PI + 1;
    if (b == U2 - 1) src[b + 1][k++] = OP_PI + 2;
    if (b == U3 - 1) src[b + 1][k++] = OP_PI + 3;
    src[b + 1][k++] = OP_S2;
    src[b + 1][k] = OP_END;
  }
  Plan p{};
  p.ok = true;
  bool used[NSLOT] = {};
  int owner[NSLOT] = {};
  int cnt = 0, tend[NT] = {}, pi[5] = {}, gi = 0, nexta = 0, nextw = 0;
  auto take = [&](int t) {
    int s = 0; while (s < NSLOT && used[s]) s++;
    if (s == NSLOT) return false;
    used[s] = true; owner[s] = t; p.slot[t] = s; return true;
  };
  for (int t = 0; t < 4; t++) { take(t); cnt += 2; tend[t] = cnt; }
  for (int b = -1; b < NB; b++) {
    for (int s = 0; s < NSLOT; s++) if (used[s] && rel_bar(owner[s]) <= b) used[s] = false;
    if (isa && b == 8 && !take(T_KVS)) p.ok = false;
    int k = 0;
    auto emit = [&](int op, int arg) { p.op[b + 1][k] = op; p.arg[b + 1][k] = arg; k++; };
    for (int i = 0; src[b + 1][i] != OP_END; i++) {
      const int op = src[b + 1][i];
      if (op < NT) { if (!take(op)) p.ok = false; cnt += 4; tend[op] = cnt; emit(op, 0); }
      else if (op < OP_PC) { cnt += 1; pi[op - OP_PI] = cnt; emit(op, 0); }
      else if (op < OP_GI) emit(op, cnt - pi[op - OP_PC]);
      else if (op == OP_GI) { cnt += 17; gi = cnt; emit(op, 0); }
      else if (op == OP_GC) emit(op, cnt - gi);
      else if (op == OP_COOP) { cnt += 1; emit(op, 0); }
      else if (op < OP_S2) emit(op, 0);
      else {
        for (int n = 0; n < (op == OP_S1 ? AONE : CAPA); n++) {
          const bool a_ok = op < OP_W && nexta < 15 && b >= (nexta < 4 ? 10 : nexta < 8 ? (isa ? 10 : U1) : nexta < 11 ? U2 : U3);
          const bool w_ok = nextw < 15 && (op >= OP_W ? nextw < op - OP_W : nextw <= nexta + (nexta < 15 && !a_ok));
          int t = 0;
          if (a_ok && (!w_ok || nexta <= nextw)) t = T_A + nexta; else if (w_ok) t = T_W + nextw; else break;
          if (!take(t)) break;
          cnt += 4; if (t == T_A + 8) cnt += 1;
          tend[t] = cnt; emit(t, 0);
          if (t >= T_A) nexta++; else nextw++;
        }
      }
    }
    p.op[b + 1][k] = OP_END;
    if (b + 1 < NB) {
      int last = 0;
      for (int t = 0; t < T_KVS; t++) if (need_bar(t) == b + 1) { if (tend[t] == 0) p.ok = false; if (tend[t] > last) last = tend[t]; }
      p.wait[b + 1] = last == 0 || cnt - last > 63 ? 63 : cnt - last;
    }
  }
  if (nexta != 15 || nextw != 15) p.ok = false;
  for (int t = T_W + 11; t < T_KVS; t++) if ((t < T_A || t > T_A + 10) && p.slot[t] == p.slot[T_W + 10]) p.ok = false;
  return p;
}
template <bool ISA> constexpr Plan PLAN = make_plan(ISA);
static_assert(PLAN<true>.ok && PLAN<false>.ok, "plan");
constexpr int nops(const Plan& p, int b) { int k = 0; while (p.op[b + 1][k] != OP_END) k++; return k; }
#define P PLAN<ISA>

template <int N> DEV void wait_vm() { asm volatile("s_waitcnt vmcnt(%0)" ::"i"(N) : "memory"); }
DEV void wait_lgkm() { asm volatile("s_waitcnt lgkmcnt(0)" ::: "memory"); }
DEV void bar() { asm volatile("s_barrier" ::: "memory"); }
DEV void bar_lgkm() { asm volatile("s_waitcnt lgkmcnt(0)\n\ts_barrier" ::: "memory"); }
DEV void sched_fence() { __builtin_amdgcn_sched_barrier(0); }
DEV int fresh_lane() {
  int l;
  asm volatile("v_mbcnt_lo_u32_b32 %0, -1, 0\n\tv_mbcnt_hi_u32_b32 %0, -1, %0" : "=v"(l));
  return l;
}
#define LANE_VARS const int lane = fresh_lane(), g = lane >> 4, r16 = lane & 15, q = (lane >> 2) & 3, p = lane & 3, key2 = (q >> 1) | ((g & 1) << 1); \
  (void)g; (void)r16; (void)q; (void)p; (void)key2;

template <int LOFF> DEV void glds(unsigned lbase, unsigned voff, rsrc_t r, unsigned soff) {
  asm volatile("s_add_u32 m0, %0, %1\n\ts_nop 0\n\tbuffer_load_dwordx4 %2, %3, %4 offen lds"
               ::"s"(lbase), "i"(LOFF), "v"(voff), "s"(r), "s"(soff) : "memory", "scc");
}
template <int LOFF> DEV void glds4(unsigned lbase, const unsigned (&v)[4], rsrc_t r, unsigned soff) {
  asm volatile("s_add_u32 m0, %0, %1\n\ts_nop 0\n\tbuffer_load_dwordx4 %2, %6, %7 offen lds\n\t"
               "buffer_load_dwordx4 %3, %6, %7 offen offset:1024 lds\n\tbuffer_load_dwordx4 %4, %6, %7 offen offset:2048 lds\n\t"
               "buffer_load_dwordx4 %5, %6, %7 offen offset:3072 lds"
               ::"s"(lbase), "i"(LOFF), "v"(v[0]), "v"(v[1]), "v"(v[2]), "v"(v[3]), "s"(r), "s"(soff) : "memory", "scc");
}
template <int LOFF> DEV void glds4_sc1(unsigned lbase, const unsigned (&v)[4], rsrc_t r, unsigned soff) {
  asm volatile("s_add_u32 m0, %0, %1\n\ts_nop 0\n\tbuffer_load_dwordx4 %2, %6, %7 offen sc1 lds\n\t"
               "buffer_load_dwordx4 %3, %6, %7 offen offset:1024 sc1 lds\n\tbuffer_load_dwordx4 %4, %6, %7 offen offset:2048 sc1 lds\n\t"
               "buffer_load_dwordx4 %5, %6, %7 offen offset:3072 sc1 lds"
               ::"s"(lbase), "i"(LOFF), "v"(v[0]), "v"(v[1]), "v"(v[2]), "v"(v[3]), "s"(r), "s"(soff) : "memory", "scc");
}
template <int LOFF> DEV void glds_dw_sc1(unsigned lbase, unsigned voff, rsrc_t r) {
  asm volatile("s_add_u32 m0, %0, %1\n\ts_nop 0\n\tbuffer_load_dword %2, %3, 0 offen sc1 lds"
               ::"s"(lbase), "i"(LOFF), "v"(voff), "s"(r) : "memory", "scc");
}
template <int LOFF> DEV void glds_dw(unsigned lbase, unsigned voff, rsrc_t r) {
  asm volatile("s_add_u32 m0, %0, %1\n\ts_nop 0\n\tbuffer_load_dword %2, %3, 0 offen lds"
               ::"s"(lbase), "i"(LOFF), "v"(voff), "s"(r) : "memory", "scc");
}
template <int LOFF> DEV void glds_dws(unsigned lbase, unsigned voff, rsrc_t r, unsigned soff) {
  asm volatile("s_add_u32 m0, %0, %1\n\ts_nop 0\n\tbuffer_load_dword %2, %3, %4 offen lds"
               ::"s"(lbase), "i"(LOFF), "v"(voff), "s"(r), "s"(soff) : "memory", "scc");
}
DEV void ld128_sc1(u32x4& d, unsigned voff, rsrc_t r) {
  asm volatile("buffer_load_dwordx4 %0, %1, %2, 0 offen sc1" : "=v"(d) : "v"(voff), "s"(r) : "memory");
}
template <int IMM> DEV void ld64_sc1(u32x2& d, unsigned voff, rsrc_t r, unsigned soff) {
  asm volatile("buffer_load_dwordx2 %0, %1, %2, %3 offen offset:%4 sc1" : "=v"(d) : "v"(voff), "s"(r), "s"(soff), "i"(IMM) : "memory");
}
DEV void ld16(unsigned& d, unsigned voff, rsrc_t r, unsigned soff) {
  asm volatile("buffer_load_ushort %0, %1, %2, %3 offen" : "=v"(d) : "v"(voff), "s"(r), "s"(soff) : "memory");
}
DEV void st128_sc1(u32x4 d, unsigned voff, rsrc_t r) { __builtin_amdgcn_raw_buffer_store_b128(d, r, voff, 0, 16); }
DEV void st64_sc1(u32x2 d, unsigned voff, rsrc_t r) { __builtin_amdgcn_raw_buffer_store_b64(d, r, voff, 0, 16); }
DEV void st32_sc1(unsigned d, unsigned voff, rsrc_t r) { __builtin_amdgcn_raw_buffer_store_b32(d, r, voff, 0, 16); }
DEV void flag_store(u32x2 tagv, unsigned voff, rsrc_t r) {
  asm volatile("s_mov_b64 exec, 1\n\tbuffer_store_dwordx2 %0, %1, %2, 0 offen sc1\n\ts_mov_b64 exec, -1" ::"v"(tagv), "v"(voff), "s"(r) : "memory");
}
DEV bool is0(int v) { asm volatile("" : "+s"(v)); return v == 0; }
template <class T> DEV void pin(T& v) { asm volatile("" : "+v"(v)); }
DEV unsigned long long tag_of(u32x2 ep) {
  const unsigned long long t = (((unsigned long long)(unsigned)__builtin_amdgcn_readfirstlane(ep[1]) << 32) | (unsigned)__builtin_amdgcn_readfirstlane(ep[0])) + 1;
  return t ? t : 1;
}
DEV bool all_tagged(u32x2 v, unsigned long long tag) {
  unsigned long long m;
  asm volatile("v_cmp_ne_u64 %0, %1, %2\n\ts_nop 1" : "=s"(m) : "s"(tag), "v"(v));
  return m == 0;
}
DEV rsrc_t mk(const void* p, unsigned bytes) { return __builtin_amdgcn_make_buffer_rsrc((void*)p, 0, bytes, 0x00020000); }

template <int L> DEV LAS char* slot_base(LAS char* p) {
  if constexpr (L + SLOT > 65536) { p += L; asm("" : "+v"(p)); return p; } else return p + L;
}
DEV s16x4 tr_half(LAS char* p) { return __builtin_amdgcn_ds_read_tr16_b64_v4i16((LAS s16x4*)p); }
DEV bf16x8 join(s16x4 a, s16x4 b) { return __builtin_bit_cast(bf16x8, __builtin_shufflevector(a, b, 0, 1, 2, 3, 4, 5, 6, 7)); }
DEV f32x4 mm(bf16x8 a, bf16x8 b, f32x4 c) { return __builtin_amdgcn_mfma_f32_16x16x32_bf16(a, b, c, 0, 0, 0); }
DEV void mma(f32x4& c, bf16x8 a, bf16x8 b) { asm volatile("v_mfma_f32_16x16x32_bf16 %0, %1, %2, %0" : "+v"(c) : "v"(a), "v"(b)); }
DEV void mma_edge() { __builtin_amdgcn_sched_barrier(0); asm volatile("s_nop 7\n\ts_nop 1"); __builtin_amdgcn_sched_barrier(0); }
DEV f32x4 mm16(s16x4 a, bf16x4 b, f32x4 c) { return __builtin_amdgcn_mfma_f32_16x16x16bf16_1k(a, __builtin_bit_cast(s16x4, b), c, 0, 0, 0); }
DEV f32x4 splat4(float v) { return f32x4{v, v, v, v}; }
DEV bf16x4 cvt4(f32x4 v) { return __builtin_convertvector(v, bf16x4); }
DEV bf16x8 cvt8(f32x4 a, f32x4 b) { return __builtin_shufflevector(cvt4(a), cvt4(b), 0, 1, 2, 3, 4, 5, 6, 7); }
DEV f32x4 unpack4(u32x2 v) {
  f32x4 r;
  r[0] = __builtin_bit_cast(float, v[0] << 16); r[1] = __builtin_bit_cast(float, v[0] & 0xffff0000u);
  r[2] = __builtin_bit_cast(float, v[1] << 16); r[3] = __builtin_bit_cast(float, v[1] & 0xffff0000u);
  return r;
}
constexpr float LOG2E = 1.4426950408889634f;
constexpr float EPS = 1e-6f;
DEV f32x4 phi4(f32x4 t) {
  typedef __attribute__((ext_vector_type(4))) int i32x4;
  constexpr int EB = __builtin_bit_cast(int, EPS);
  const f32x4 y = __builtin_elementwise_fma(t, f32x4{LOG2E, LOG2E, LOG2E, LOG2E}, f32x4{-EPS * LOG2E, -EPS * LOG2E, -EPS * LOG2E, -EPS * LOG2E});
  f32x4 e;
  for (int i = 0; i < 4; i++) e[i] = __builtin_amdgcn_fmed3f(__builtin_amdgcn_exp2f(y[i]), 0.f, 1.f);
  return __builtin_bit_cast(f32x4, __builtin_elementwise_max(__builtin_bit_cast(i32x4, t), i32x4{EB, EB, EB, EB})) + e;
}
DEV f32x4 gelu4(f32x4 u) {
  constexpr float K2 = -2.f * 0.7978845608028654f * LOG2E, K1 = K2 * 0.044715f;
  f32x4 t = u * __builtin_elementwise_fma(u * u, f32x4{K1, K1, K1, K1}, f32x4{K2, K2, K2, K2}), r;
  for (int i = 0; i < 4; i++) r[i] = __builtin_amdgcn_exp2f(t[i]);
  r += 1.f;
  for (int i = 0; i < 4; i++) r[i] = __builtin_amdgcn_rcpf(r[i]);
  return u * r;
}

template <int N> DEV void poll_check(u32x2& pv, unsigned voff0, rsrc_t r, unsigned long long tag, u32x2 tagv, unsigned word, bool first) {
  asm volatile("s_waitcnt vmcnt(%1)" : "+v"(pv) : "i"(N) : "memory");
  const bool up = all_tagged(pv, tag);
  if (first) {
    {
#pragma unroll 1
      for (int it = 0; it < (up ? 0 : 1 << 17); it++) {
        __builtin_amdgcn_s_sleep(SLEEP);
        u32x2 q;
        asm volatile("buffer_load_dwordx2 %0, %1, %2, 0 offen sc1\n\ts_waitcnt vmcnt(0)" : "=&v"(q) : "v"(voff0), "s"(r) : "memory");
        if (all_tagged(q, tag)) break;
      }
    }
    asm volatile("ds_write_b64 %0, %1" ::"v"(word), "v"(tagv) : "memory");
  } else if (!up) {
#pragma unroll 1
    for (int it = 0; it < (1 << 20); it++) {
      u32x2 q;
      asm volatile("ds_read_b64 %0, %1\n\ts_waitcnt lgkmcnt(0)" : "=&v"(q) : "v"(word) : "memory");
      if (all_tagged(q, tag)) break;
      __builtin_amdgcn_s_sleep(1);
    }
  }
}

template <class Fn, int... I> DEV void static_for_impl(Fn&& f, std::integer_sequence<int, I...>) { (f(IC<I>{}), ...); }
template <int N, class Fn> DEV void static_for(Fn&& f) { static_for_impl(f, std::make_integer_sequence<int, N>{}); }

#define STAMP(i) do {} while (0)
#define DIAG_ARG
#define DIAG_PASS
#define DIAG_END

DEV void gather_issue(u32x2 (&gk)[16], u32x4& gz, unsigned vg, unsigned vz, rsrc_t r_ws) {
      ld128_sc1(gz, vz, r_ws);
      static_for<16>([&](auto Qc) { constexpr int pp = Qc.value; ld64_sc1<(pp & 7) * 512>(gk[pp], vg, r_ws, (pp >> 3) * 4096); });
}
template <int arg> DEV void gather_sum(u32x2 (&gk)[16], u32x4& gz, LAS char* kdst, LAS char* zdst) {
      asm volatile("s_waitcnt vmcnt(%9)" : "+v"(gk[0]), "+v"(gk[1]), "+v"(gk[2]), "+v"(gk[3]), "+v"(gk[4]), "+v"(gk[5]), "+v"(gk[6]), "+v"(gk[7]), "+v"(gz) : "i"(arg + 8) : "memory");
      f32x4 z = __builtin_bit_cast(f32x4, gz);
      for (int i = 0; i < 4; i++) {
        z[i] += __builtin_amdgcn_update_dpp(0.f, z[i], 0x124, 0xf, 0xf, false);
        z[i] += __builtin_amdgcn_update_dpp(0.f, z[i], 0x128, 0xf, 0xf, false);
        const unsigned za = __float_as_uint(z[i]);
        auto s16 = __builtin_amdgcn_permlane16_swap(za, za, false, false);
        const unsigned zb = __float_as_uint(__uint_as_float(s16[0]) + __uint_as_float(s16[1]));
        auto s32 = __builtin_amdgcn_permlane32_swap(zb, zb, false, false);
        z[i] = __uint_as_float(s32[0]) + __uint_as_float(s32[1]);
      }
      *(LAS bf16x4*)zdst = cvt4(z);
      f32x4 s = unpack4(gk[0]);
      for (int i = 1; i < 8; i++) s += unpack4(gk[i]);
      asm volatile("s_waitcnt vmcnt(%8)" : "+v"(gk[8]), "+v"(gk[9]), "+v"(gk[10]), "+v"(gk[11]), "+v"(gk[12]), "+v"(gk[13]), "+v"(gk[14]), "+v"(gk[15]) : "i"(arg) : "memory");
      for (int i = 8; i < 16; i++) s += unpack4(gk[i]);
      *(LAS bf16x4*)kdst = cvt4(s);
}

struct Geo { int rg, mem, rt, ct, c2, rot, idx; bool half2; unsigned long long t0; };
#define HOLD(id) do {} while (0)

template <bool ISA>
DEV void loader(rsrc_t r_x, rsrc_t r_w, const void* wo, const void* w2, const void* b2, const void* gw, char* ws, LAS char* lds,
                const Geo G, int lw DIAG_ARG) {
  constexpr bool in_compute = false; (void)in_compute;
  constexpr unsigned WSTR = ISA ? N3_ * 2 : F_ * 2;
  const int lane = threadIdx.x & 63;
  const rsrc_t r_w2 = mk(w2, F_ * D_ * 2), r_wo = mk(wo, D_ * D_ * 2), r_ws = mk(ws, W_END);
  const unsigned lds0 = (unsigned)(size_t)lds, lbase = lds0 + lw * 4096;
  const int r8 = lane >> 3, pc = lane & 7, k23 = (lane >> 4) & 3;

  unsigned vx[4], vwd[4], vs[4], vv[4], va[4];
  for (int e = 0; e < 4; e++) {
    vx[e] = (G.rg * 128 + 32 * lw + 8 * e + r8) * 1024 + ((pc ^ k23 ^ ((e & 1) << 2)) << 4) - e * 1024;
    vwd[e] = (16 * lw + 4 * e + (r8 & 3)) * WSTR + (ISA ? (512 + 64 * G.mem) * 2 : 256 * G.mem) + ((pc ^ ((k23 & 1) << 1) ^ ((e >> 1) << 2)) << 4)
           + (lane >> 5) * (ISA ? 1024 : 128) - e * 1024;
    const int cs = pc ^ ((k23 & 1) << 1) ^ ((e & 1) << 2);
    vs[e] = (G.half2 && cs >= 4) ? OOB : (32 * (lw & 1) + 8 * e + r8) * WSTR + G.c2 * 2 + cs * 16 - e * 1024;
    vv[e] = (32 * (lw & 1) + 8 * e + r8) * 1024 + G.ct * 128 + ((pc ^ (k23 << 1)) << 4) - e * 1024;
    va[e] = W_A2 + (G.rt * 4 + 2 * (lw & 1) + (e >> 1)) * 60 * 1024 + (e & 1) * 1024 + lane * 16 - e * 1024;
  }
  const int rot = G.rot, rot2 = (G.ct + (G.rt & 3)) & 3, rot3 = (G.ct + (G.rt & 3)) % 3;
  auto kstep = [&](auto Uc, int hw) -> unsigned {
    constexpr int u = Uc.value;
    if constexpr (u < 8) return (((u + rot2) & 3) * 2 + hw) * 8 + (u >> 2) * 4;
    else if constexpr (u < 11) { const int v = u + rot3; return (v > 10 ? v - 3 : v) * 8 + hw * 4; }
    else return (11 + ((u - 11 + rot2) & 3)) * 8 + hw * 4;
  };
  const int piece = G.mem * 4 + lw;
  const bool gate = !ISA && G.mem == 7;
  u32x2 ep, tagv, pv, gk[16];
  unsigned long long tag;
  unsigned gq[4];
  u32x4 gz;
  unsigned vpoll[5], vword[5], vgate = W_G + (G.rt * 64 + lane) * 4, vg = W_KV + piece * 8192 + lane * 8, vz = W_ZP + piece * 1024 + lane * 16;
  for (int k = 0; k < 4; k++) vpoll[k] = W_FH + (k * 32 + G.rt) * FSTR + (lane & 15) * FL1;
  if (ISA) vpoll[0] += ((lane >> 4) & 1) * 32 * FSTR;
  vpoll[4] = W_FK + (lw == 0 ? piece + (lane >> 4) : piece) * FSTR + (lane & 15) * FL1;
  for (int k = 0; k < 5; k++) vword[k] = lds0 + L_POLL + k * 8;
  const bool co2 = G.idx < 23;
  const rsrc_t r_co = mk(co2 ? w2 : wo, co2 ? F_ * D_ * 2 : D_ * D_ * 2);
  unsigned vco = lane < 60 ? ((co2 ? G.idx * 60 : (G.idx - 23) * 57) + lw * 15 + (lane >> 2)) * 1024 + ((G.ct & 4) + (lane & 3)) * 128 : OOB;
  LAS char *kdst = lds + L_KVA + lw * 512 + lane * 8, *zdst = lds + L_ZA + lw * 32 + (lane & 3) * 8;
  LAS char* gcol = lds + (32 * lw + lane) * 128 + ((2 ^ ((lane >> 1) & 1) ^ (((lane >> 3) & 1) << 1)) << 5);

  auto run = [&](auto Oc, auto Ac) {
    constexpr int n = Oc.value, arg = Ac.value;
    if constexpr (n < 16) {
      constexpr int L = P.slot[n] * SLOT;
      const unsigned ch = ((n >> 1) + rot) & 7;
      if constexpr ((n & 1) == 0) glds4<L>(lbase, vx, r_x, ch * 128); else glds4<L>(lbase, vwd, r_w, ch * 64 * WSTR);
    } else if constexpr (n < T_W) {
      glds4<P.slot[n] * SLOT>(lbase, vs, r_w, ((2 * (n - 16) + (lw >> 1) + rot) & 7) * 64 * WSTR);
    } else if constexpr (n < T_A) {
      constexpr int u = n - T_W;
      if constexpr (u < 11) glds4<P.slot[n] * SLOT>(lbase, vv, r_w2, kstep(IC<u>{}, lw >> 1) * 16384);
      else glds4<P.slot[n] * SLOT>(lbase, vv, r_wo, (kstep(IC<u>{}, lw >> 1) - 88) * 16384);
    } else if constexpr (n < NT) {
      glds4_sc1<P.slot[n] * SLOT>(lbase, va, r_ws, kstep(IC<n - T_A>{}, lw >> 1) * 512);
      if constexpr (n == T_A + 8) glds_dw_sc1<L_G>(lds0, vgate, r_ws);
    } else if constexpr (n < OP_PC) {
      ld64_sc1<0>(pv, vpoll[n - OP_PI], r_ws, 0);
    } else if constexpr (n < OP_GI) {
      poll_check<arg>(pv, vpoll[n - OP_PC], r_ws, tag, tagv, vword[n - OP_PC], is0(lw));
      STAMP(108 + n - OP_PC);
    } else if constexpr (n == OP_GI) {
      gather_issue(gk, gz, vg, vz, r_ws);
    } else if constexpr (n == OP_GC) {
      gather_sum<arg>(gk, gz, kdst, zdst);
      STAMP(39);
    } else if constexpr (n == OP_COOP) {
      glds_dw<L_DUMMY>(lds0, vco, r_co);
    } else if constexpr (n == OP_SLEEP) {
      __builtin_amdgcn_s_sleep(ISA ? DLY_A : DLY_F);
    } else if constexpr (n == OP_MISC) {
      ld64_sc1<0>(ep, W_EPOCH, r_ws, 0);
      if (lw == 0 && lane < 8) glds<L_B2>(lds0, G.ct * 128 + lane * 16, mk(b2, D_ * 2), 0);
      if constexpr (!ISA)
        for (int t = 0; t < 4; t++) ld16(gq[t], (32 * (lw & 1) + (lane & 31)) * 2, mk(gw, D_ * 2), ((2 * t + (lw >> 1) + rot) & 7) * 128);
    }
  };
  auto run_seg = [&](auto Bc) {
    static_for<nops(P, Bc.value)>([&](auto Kc) { run(IC<P.op[decltype(Bc)::value + 1][Kc.value]>{}, IC<P.arg[decltype(Bc)::value + 1][Kc.value]>{}); });
  };
  auto patch_gate = [&](auto Tc) {
    *(LAS unsigned short*)slot_base<P.slot[16 + Tc.value] * SLOT>(gcol) = (unsigned short)gq[Tc.value];
  };

  __builtin_amdgcn_s_setprio(1);
  run_seg(IC<-1>{});
  for (int k = 0; k < 5; k++) { if (ISA || k < 4) { pin(vpoll[k]); pin(vword[k]); } }
  for (int e = 0; e < 4; e++) { pin(vx[e]); pin(vwd[e]); pin(vs[e]); pin(vv[e]); pin(va[e]); }
  pin(vgate); pin(gcol); pin(vco);
  if constexpr (ISA) { pin(vg); pin(vz); pin(kdst); pin(zdst); }
  if (lw == 0 && lane < 5) *(LAS u32x2*)(lds + L_POLL + lane * 8) = u32x2{0, 0};
  wait_lgkm();
  STAMP(122);
  wait_vm<P.wait[0] + (ISA ? 1 : 5)>();
  bar();

  static_for<NB>([&](auto Bc) {
    constexpr int b = Bc.value;
    {
      STAMP(4 * b);
      run_seg(Bc);
      if constexpr ((b == 23 || b == 25) && !ISA) {
        if (blockIdx.x == 255 && lw == 3) {
          const unsigned f0 = W_FH + (lane >> 3) * 2 * FSTR + (lane & 7) * 2 * FL1, f1 = W_FK + (lane >> 4) * 4 * FSTR + (lane & 15) * FL1;
#pragma unroll 1
          for (int it = 0; it < (1 << 17); it++) {
            if (b == 23 || it > 0)
              asm volatile("buffer_load_dwordx2 v[248:249], %0, %2, 0 offen sc1\n\tbuffer_load_dwordx2 v[250:251], %0, %2, %3 offen sc1\n\t"
                           "buffer_load_dwordx2 v[252:253], %1, %2, 0 offen sc1\n\tbuffer_load_dwordx2 v[254:255], %1, %2, %3 offen sc1"
                           :: "v"(f0), "v"(f1), "s"(r_ws), "s"(16 * FSTR)
                           : "memory", "v248", "v249", "v250", "v251", "v252", "v253", "v254", "v255");
            if (b == 23) break;
            unsigned long long m0, m1, m2, m3;
            asm volatile("s_waitcnt vmcnt(0)\n\tv_cmp_ne_u64 %0, %4, v[248:249]\n\tv_cmp_ne_u64 %1, %4, v[250:251]\n\t"
                         "v_cmp_ne_u64 %2, %4, v[252:253]\n\tv_cmp_ne_u64 %3, %4, v[254:255]\n\ts_nop 1"
                         : "=&s"(m0), "=&s"(m1), "=&s"(m2), "=&s"(m3) : "s"(tag) : "memory");
            if ((m0 | m1 | m2 | m3) == 0) break;
          }
          if (b == 25) flag_store(tagv, W_EPOCH, r_ws);
        }
      }
      if constexpr (b != 9) STAMP(4 * b + 2);
      constexpr int nb = b + 1;
      if constexpr (nb < NB) {
        if constexpr (nb == 2) {
          if constexpr (ISA) asm volatile("s_waitcnt vmcnt(%1)" : "+v"(ep) : "i"(P.wait[nb]) : "memory");
          else asm volatile("s_waitcnt vmcnt(%5)" : "+v"(ep), "+v"(gq[0]), "+v"(gq[1]), "+v"(gq[2]), "+v"(gq[3]) : "i"(P.wait[nb]) : "memory");
          tag = tag_of(ep);
          tagv[0] = (unsigned)tag; tagv[1] = (unsigned)(tag >> 32);
          pin(tagv);
        } else if constexpr (nb == 1) wait_vm<P.wait[nb] + (ISA ? 1 : 5)>();
        else wait_vm<P.wait[nb]>();
        if constexpr (!ISA && nb == (NOPRE ? 9 : 8)) { if (gate) { if (lane < 32) patch_gate(IC<0>{}); wait_lgkm(); } }
        if constexpr (!ISA && nb == 9) { if (gate) { if (lane < 32) { patch_gate(IC<1>{}); patch_gate(IC<2>{}); patch_gate(IC<3>{}); } wait_lgkm(); } }
        if constexpr (b != 9) STAMP(4 * b + 3);
        if constexpr (nb == 11) bar_lgkm(); else bar();
      }
    }
  });
  if constexpr (ISA) asm volatile(".Lgmix_end0:"); else asm volatile(".Lgmix_end1:");
  DIAG_END
}

template <bool ISA>
DEV void compute(const void* b1, void* out, char* ws, LAS char* lds, const Geo G, int wi DIAG_ARG) {
  constexpr bool in_compute = true; (void)in_compute;
  const rsrc_t r_ws = mk(ws, W_END);
  LAS char *xb[2], *wdb[4], *cur[4];
  bf16x8 xf[2][16];
  s16x4 wlo[LA + 1], whi[LA + 1];
  f32x4 acc1[8][2], acc2[4][2];
  u32x2 ep;
  unsigned long long tag = 0;
  {
    LANE_VARS
    ld64_sc1<0>(ep, W_EPOCH, r_ws, 0);
    if constexpr (!ISA) glds_dws<L_B1 + 256>((unsigned)(size_t)lds + wi * 1024, lane < (G.half2 ? 16 : 32) ? lane * 4 : OOB, mk(b1, F_ * 2), G.c2 * 2);
    for (int k = 0; k < 2; k++) xb[k] = lds + (32 * wi + r16) * 128 + (((4 * k + g) ^ (r16 >> 1)) << 4);
    for (int c = 0; c < 4; c++) wdb[c] = lds + g * 2048 + q * 128 + ((c ^ key2) << 5) + 8 * p;
    if constexpr (ISA) {
      for (int c = 0; c < 8; c++) for (int sb = 0; sb < 2; sb++) acc1[c][sb] = splat4(c < 4 ? EPS : 0.f);
      wait_vm<5>();
    } else {
      wait_vm<10>();
      for (int c = 0; c < 8; c++) { acc1[c][0] = unpack4(*(LAS u32x2*)(lds + L_B1 + wi * 1024 + c * 32 + g * 8)); acc1[c][1] = acc1[c][0]; }
    }
  }
  const unsigned sst = W_A2 + (G.rg * 8 + 2 * wi) * 60 * 1024;
  auto flag = [&](unsigned off) { flag_store(u32x2{(unsigned)tag, (unsigned)(tag >> 32)}, off, r_ws); };
  const unsigned fh = W_FH + (G.rg * 2 + (wi >> 1)) * FSTR + (G.mem * 2 + (wi & 1)) * FL1;

  auto rd = [&](auto Sc, auto Hc) {
    constexpr int s = Sc.value, h = Hc.value;
    if constexpr (s < 128) {
      constexpr int t = s >> 4, kb = (s >> 3) & 1, cb = s & 7;
      if constexpr ((s & 15) == 0 && h == 0) {
        if constexpr (t == 1) {
          asm volatile("s_waitcnt vmcnt(0)" : "+v"(ep) :: "memory");
          tag = tag_of(ep);
        }
        bar();
        STAMP(4 * t);
        if constexpr (t == 1) HOLD(1);
        for (int c = 0; c < 4; c++) cur[c] = slot_base<P.slot[2 * t + 1] * SLOT>(wdb[c]);
      }
      if constexpr (cb == 0) xf[h][2 * t + kb] = *(LAS bf16x8*)(xb[kb] + P.slot[2 * t] * SLOT + h * 2048);
      (h ? whi : wlo)[s % (LA + 1)] = tr_half(cur[cb & 3] + kb * 8192 + (cb >> 2) * 512 + h * 1024);
    } else if constexpr (s < 192) {
      constexpr int tt = (s - 128) >> 4, kb = (s >> 2) & 3, cb = s & 3;
      if constexpr ((s & 15) == 0 && h == 0) {
        if constexpr (tt == 0) {
          if constexpr (!NOPRE) bar();
          STAMP(32);
          LANE_VARS
          for (int c = 0; c < 4; c++) wdb[c] = lds + (8 * g + q) * 128 + ((c ^ key2) << 5) + 8 * p;
        }
        for (int c = 0; c < 4; c++) cur[c] = slot_base<P.slot[16 + tt] * SLOT>(wdb[c]);
      }
      (h ? whi : wlo)[s % (LA + 1)] = tr_half(cur[cb] + kb * 4096 + h * 512);
    }
  };
  auto gelu_store = [&](f32x4 a, f32x4 b, unsigned voff) { st128_sc1(__builtin_bit_cast(u32x4, cvt8(gelu4(a), gelu4(b))), voff, r_ws); };

  sched_fence();
  for (int c = 0; c < 8; c++) for (int sb = 0; sb < 2; sb++) pin(acc1[c][sb]);
  if constexpr (!ISA) wait_vm<6>();
  static_for<LA>([&](auto Sc) { rd(Sc, IC<0>{}); rd(Sc, IC<1>{}); });
  mma_edge();
  static_for<192>([&](auto Sc) {
    constexpr int s = Sc.value;
    const bf16x8 wv = join(wlo[s % (LA + 1)], whi[s % (LA + 1)]);
    static_for<2>([&](auto Hc) {
      constexpr int sb = Hc.value, s_ = decltype(Sc)::value;
      if constexpr (s_ >= 128) mma(acc2[s_ & 3][sb], wv, xf[sb][(s_ - 128) >> 2]);
      else if constexpr (ISA) mma(acc1[s_ & 7][sb], xf[sb][s_ >> 3], wv);
      else mma(acc1[s_ & 7][sb], wv, xf[sb][s_ >> 3]);
      sched_fence();
      if constexpr (!(NOPRE && s_ < 128 && s_ + LA >= 128)) rd(IC<s_ + LA>{}, Hc);
      else if constexpr (s_ + LA == 128 && sb == 0) bar();
      sched_fence();
    });
    if constexpr (s == 127) {
      mma_edge();
      HOLD(2);
      STAMP(33);
      LANE_VARS
      if constexpr (ISA) {
        const bf16x8 ones = {1.f, 1.f, 1.f, 1.f, 1.f, 1.f, 1.f, 1.f};
        LAS char* kp = lds + P.slot[T_KVS] * SLOT + wi * 4096 + lane * 16;
        f32x4 kv[4], z[4];
#pragma unroll
        for (int h = 0; h < 4; h++) {
          const bf16x8 pk = cvt8(phi4(acc1[h][0]), phi4(acc1[h][1]));
          if (h > 0) *(LAS f32x4*)(kp + (h - 1) * 1024) = kv[h - 1];
          kv[h] = mm(pk, cvt8(acc1[4 + h][0], acc1[4 + h][1]), splat4(0.f));
          z[h] = mm(pk, ones, splat4(0.f));
          sched_fence();
        }
        for (int c = 0; c < 4; c++) for (int sb = 0; sb < 2; sb++) { acc2[c][sb] = splat4(EPS); pin(acc2[c][sb]); }
        sched_fence();
        *(LAS f32x4*)(kp + 3 * 1024) = kv[3];
        const f32x4 zs = r16 & 2 ? (r16 & 1 ? z[3] : z[2]) : (r16 & 1 ? z[1] : z[0]);
        if (r16 < 4) *(LAS f32x4*)(lds + L_ZP + wi * 256 + r16 * 64 + g * 16) = zs;
      } else {
        for (int un = 0; un < 4; un++) for (int sb = 0; sb < 2; sb++)
          gelu_store(acc1[2 * un][sb], acc1[2 * un + 1][sb], sst + (sb * 60 + 4 * G.mem + un) * 1024 + lane * 16);
        for (int c = 0; c < 4; c++) { acc2[c][0] = unpack4(*(LAS u32x2*)(lds + L_B1 + wi * 1024 + 256 + c * 32 + g * 8)); acc2[c][1] = acc2[c][0]; }
      }
      for (int c = 0; c < 4; c++) for (int sb = 0; sb < 2; sb++) pin(acc2[c][sb]);
      HOLD(9);
      STAMP(34);
      bar_lgkm();
      STAMP(36);
      if constexpr (NOPRE) { static_for<LA>([&](auto Kc) { rd(IC<128 + Kc.value>{}, IC<0>{}); rd(IC<128 + Kc.value>{}, IC<1>{}); }); sched_fence(); }
      if constexpr (ISA) {
        LAS char* kp = lds + P.slot[T_KVS] * SLOT + wi * 1024 + lane * 16;
        f32x4 sk = *(LAS f32x4*)kp, z = *(LAS f32x4*)(lds + L_ZP + wi * 64 + p * 16);
        for (int i = 1; i < 4; i++) { sk += *(LAS f32x4*)(kp + i * 4096); z += *(LAS f32x4*)(lds + L_ZP + i * 256 + wi * 64 + p * 16); }
        const int pr = (G.mem * 4 + wi) * 16 + G.rg;
        st64_sc1(__builtin_bit_cast(u32x2, cvt4(sk)), W_KV + pr * 512 + lane * 8, r_ws);
        st128_sc1(__builtin_bit_cast(u32x4, z), lane < 4 ? W_ZP + pr * 64 + lane * 16 : OOB, r_ws);
        STAMP(37);
      }
      mma_edge();
    }
    if constexpr (!ISA && s == FA_STEP) { wait_vm<4>(); flag(fh); STAMP(38); sched_fence(); }
    if constexpr (s == REL_STEP) { bar(); sched_fence(); }
    if constexpr (s == (ISA ? FBA : FB_STEP)) {
      wait_vm<ISA ? 0 : 1>();
      flag(ISA ? W_FK + (G.mem * 4 + wi) * FSTR + G.rg * FL1 : fh + 32 * FSTR);
      STAMP(39);
      sched_fence();
    }
  });
  mma_edge();
  HOLD(6);
  STAMP(45);
  LANE_VARS
  if constexpr (ISA) {
    bf16x4 pq[4][2];
    for (int h = 0; h < 4; h++) for (int sb = 0; sb < 2; sb++) pq[h][sb] = cvt4(phi4(acc2[h][sb]));
    sched_fence();
    STAMP(40);
    bar();
    STAMP(41);
    f32x4 num[4][2], den[4][2];
    for (int h = 0; h < 4; h++) {
      const s16x4 ku = *(LAS s16x4*)(lds + L_KVA + h * 512 + lane * 8), zu = *(LAS s16x4*)(lds + L_ZA + h * 32 + g * 8);
      for (int sb = 0; sb < 2; sb++) { num[h][sb] = mm16(ku, pq[h][sb], splat4(0.f)); den[h][sb] = mm16(zu, pq[h][sb], splat4(EPS)); }
    }
    for (int hp = 0; hp < 2; hp++) for (int sb = 0; sb < 2; sb++)
      st128_sc1(__builtin_bit_cast(u32x4, cvt8(num[2 * hp][sb] * __builtin_amdgcn_rcpf(den[2 * hp][sb][0]),
                                               num[2 * hp + 1][sb] * __builtin_amdgcn_rcpf(den[2 * hp + 1][sb][0]))),
                sst + (sb * 60 + 44 + 2 * G.mem + hp) * 1024 + lane * 16, r_ws);
    STAMP(42);
  } else {
    const unsigned vo = sst + (G.c2 >> 5) * 1024 + lane * 16;
    for (int sb = 0; sb < 2; sb++) gelu_store(acc2[0][sb], acc2[1][sb], vo + sb * 60 * 1024);
    sched_fence();
    bar();
    sched_fence();
    if (!G.half2) { for (int sb = 0; sb < 2; sb++) gelu_store(acc2[2][sb], acc2[3][sb], vo + (sb * 60 + 1) * 1024); }
    else if (G.mem == 7) {
      for (int sb = 0; sb < 2; sb++) {
        const float gv = __builtin_amdgcn_rcpf(1.f + __builtin_amdgcn_exp2f(-LOG2E * acc2[2][sb][0]));
        st32_sc1(__float_as_uint(gv), lane < 16 ? W_G + (G.rg * 128 + 32 * wi + 16 * sb + lane) * 4 : OOB, r_ws);
        st128_sc1(u32x4{0, 0, 0, 0}, vo + (sb * 60 + 1) * 1024, r_ws);
      }
    }
  }
  STAMP(47);
  HOLD(3);
  sched_fence();

  const int wr = wi >> 1, wc = wi & 1;
  f32x4 accb[2][2] = {}, acca[2][2] = {}, bgn[2][2];
  float gv[2];
  bf16x8 fa[LA2 + 1][2];
  s16x4 flo[LA2 + 1][2], fhi[LA2 + 1][2];
  LAS char* const ab = lds + wr * 4096 + lane * 16;
  LAS char* wb[2];
  for (int c = 0; c < 2; c++) wb[c] = lds + (4 * g + q) * 128 + (((2 * wc + c) ^ key2) << 5) + 8 * p;
  LAS char *ca, *cw[2];
  auto rd2 = [&](auto Vc, auto Jc) {
    constexpr int v = Vc.value, j = Jc.value, u = v >> 2, kk = v & 3, i = v % (LA2 + 1);
    if constexpr (v < 60) {
      if constexpr (kk == 0 && j == 0) {
        bar_lgkm();
        STAMP(48 + 4 * u);
        if constexpr (u == 8) HOLD(4);
        if constexpr (u == 4) HOLD(7);
        if constexpr (u == 11) HOLD(8);
        ca = slot_base<P.slot[T_A + u] * SLOT>(ab);
        for (int c = 0; c < 2; c++) cw[c] = slot_base<P.slot[T_W + u] * SLOT>(wb[c]);
      }
      if constexpr (j == 0) { flo[i][0] = tr_half(cw[0] + kk * 4096); fhi[i][0] = tr_half(cw[0] + kk * 4096 + 2048); }
      if constexpr (j == 1) fa[i][0] = *(LAS bf16x8*)(ca + (kk >> 1) * 8192 + (kk & 1) * 1024);
      if constexpr (j == 2) fa[i][1] = *(LAS bf16x8*)(ca + (kk >> 1) * 8192 + (kk & 1) * 1024 + 2048);
      if constexpr (j == 3) { flo[i][1] = tr_half(cw[1] + kk * 4096); fhi[i][1] = tr_half(cw[1] + kk * 4096 + 2048); }
    }
  };
  for (int c = 0; c < 2; c++) for (int sb = 0; sb < 2; sb++) { pin(accb[c][sb]); pin(acca[c][sb]); }
  static_for<LA2>([&](auto Vc) { static_for<4>([&](auto Jc) { rd2(Vc, Jc); }); });
  mma_edge();
  static_for<60>([&](auto Vc) {
    constexpr int v = Vc.value;
    static_for<4>([&](auto Jc) {
      constexpr int j = Jc.value, v_ = decltype(Vc)::value, i = v_ % (LA2 + 1), c = j >> 1, sb = j & 1;
      mma(v_ < 44 ? accb[c][sb] : acca[c][sb], join(flo[i][c], fhi[i][c]), fa[i][sb]);
      sched_fence();
      rd2(IC<v_ + LA2>{}, Jc);
      sched_fence();
    });
    if constexpr (v == 36) {
      for (int sb = 0; sb < 2; sb++) gv[sb] = *(LAS float*)(lds + L_G + (32 * wr + 16 * sb + r16) * 4);
      for (int c = 0; c < 2; c++) {
        const f32x4 bb = unpack4(*(LAS u32x2*)(lds + L_B2 + wc * 64 + c * 32 + g * 8));
        for (int sb = 0; sb < 2; sb++) { bgn[c][sb] = bb * (1.f - gv[sb]); pin(bgn[c][sb]); }
      }
      sched_fence();
    }
    if constexpr (v == FL_STEP) { wait_vm<0>(); flag(fh + (ISA ? 96 : 64) * FSTR); STAMP(43); sched_fence(); }
  });
  mma_edge();
  HOLD(5);
  STAMP(123);
  {
    LAS char* img = lds + P.slot[T_W + 10] * SLOT;
    for (int c = 0; c < 2; c++) for (int sb = 0; sb < 2; sb++)
      *(LAS bf16x4*)(img + (32 * wr + 16 * sb + r16) * 144 + wc * 64 + c * 32 + g * 8) = cvt4(acca[c][sb] * gv[sb] + (accb[c][sb] * (1.f - gv[sb]) + bgn[c][sb]));
    bar_lgkm();
    const rsrc_t r_out = mk(out, S_ * D_ * 2);
    for (int e = 0; e < 2; e++) {
      const int row = 16 * wi + 8 * e + (lane >> 3);
      const u32x4 v = *(LAS u32x4*)(img + row * 144 + (lane & 7) * 16);
      __builtin_amdgcn_raw_buffer_store_b128(v, r_out, (G.rt * 64 + row) * 1024 + G.ct * 128 + (lane & 7) * 16, 0, 0);
    }
  }
  if constexpr (ISA) asm volatile(".Lgmix_end2:"); else asm volatile(".Lgmix_end3:");
  DIAG_END
}

__global__ __launch_bounds__(512, 2) void fused_kernel(const void* x, const void* wqkv, const void* wo, const void* w1, const void* b1,
                                                       const void* w2, const void* b2, const void* gw, void* out, char* ws) {
  extern __shared__ __attribute__((aligned(16))) char smem[];
  LAS char* lds = (LAS char*)smem;
  const unsigned tid = threadIdx.x, bid = blockIdx.x;
  {
    unsigned long long pc;
    unsigned len, t;
    asm volatile("s_getpc_b64 %0\n.Lgmix_begin:\n\t"
                 "s_mov_b32 %1, .Lgmix_end0-.Lgmix_begin\n\ts_mov_b32 %2, .Lgmix_end1-.Lgmix_begin\n\ts_max_i32 %1, %1, %2\n\t"
                 "s_mov_b32 %2, .Lgmix_end2-.Lgmix_begin\n\ts_max_i32 %1, %1, %2\n\ts_mov_b32 %2, .Lgmix_end3-.Lgmix_begin\n\ts_max_i32 %1, %1, %2"
                 : "=&s"(pc), "=&s"(len), "=&s"(t) :: "scc");
    glds_dw<L_DUMMY>((unsigned)(size_t)lds, (tid & 62) == 0 ? ((bid >> 3) * 16 + (tid >> 5) + (tid & 1)) * 128 : OOB, mk((const void*)pc, len & ~3u));
    sched_fence();
  }
  const unsigned lb = (unsigned)(size_t)lds + (__builtin_amdgcn_readfirstlane(tid) << 4);
  const int rg = ((bid & 4) << 1) + (bid >> 5), rot = ROTM == 8 ? (rg + ((bid >> 2) & 6)) & 7 : ROTM == 4 ? ((rg + (bid >> 3)) & 3) * 2 : ((rg + (bid >> 3)) & 1) * 4, rot1 = (rot + 1) & 7;
  const rsrc_t r_x = mk(x, S_ * D_ * 2);
  {
    const unsigned vx = ((tid >> 3) << 10) | (((tid ^ (tid >> 4)) & 7) << 4), sx = rg << 17;
    glds_dws<L_B1>(lb, (tid & 63) << 2, mk(b1, F_ * 2), (((bid & 3) << 1) + ((bid >> 3) & 1)) << 8);
    glds<0>(lb, vx, r_x, sx + rot * 128); glds<8192>(lb, vx, r_x, sx + rot * 128 + 65536);
    sched_fence();
    const bool isa_ = !(bid & 16);
    const unsigned wstr = isa_ ? N3_ * 2 : F_ * 2, mem = ((bid & 3) << 1) + ((bid >> 3) & 1);
    const rsrc_t r_w = mk(isa_ ? wqkv : w1, D_ * wstr);
    const unsigned vw = (((tid >> 4) & ~3u) | ((tid >> 3) & 3)) * wstr + (((tid ^ ((tid >> 3) & 2) ^ ((tid >> 5) & 4)) & 7) << 4) + (((tid >> 5) & 1) << (isa_ ? 10 : 7));
    const unsigned sw = isa_ ? 1024 + 128 * mem : 256 * mem;
    glds<SLOT>(lb, vw, r_w, sw + rot * 64 * wstr); glds<SLOT + 8192>(lb, vw, r_w, sw + (rot * 64 + 32) * wstr);
    glds<2 * SLOT>(lb, vx, r_x, sx + rot1 * 128); glds<2 * SLOT + 8192>(lb, vx, r_x, sx + rot1 * 128 + 65536);
    glds<3 * SLOT>(lb, vw, r_w, sw + rot1 * 64 * wstr); glds<3 * SLOT + 8192>(lb, vw, r_w, sw + (rot1 * 64 + 32) * wstr);
    sched_fence();
  }
  const int xcd = bid & 7, idx = bid >> 3, sel = idx & 3;
  const int w = __builtin_amdgcn_readfirstlane(tid >> 6);
  const bool isa = sel < 2;
  Geo G;
  G.rg = rg;
  G.mem = 2 * (xcd & 3) + (sel & 1);
  G.rt = (xcd >> 1) * 8 + (idx >> 2);
  G.ct = (xcd & 1) * 4 + (idx & 3);
  G.idx = idx;
  G.rot = rot;
  const rsrc_t r_w = mk(isa ? wqkv : w1, D_ * (isa ? N3_ * 2 : F_ * 2));
  if (isa) {
    G.c2 = 64 * G.mem; G.half2 = false;
    if (w >= 4) loader<true>(r_x, r_w, wo, w2, b2, gw, ws, lds, G, w - 4 DIAG_PASS); else compute<true>(b1, out, ws, lds, G, w DIAG_PASS);
  } else {
    G.c2 = G.mem < 3 ? 1024 + 64 * G.mem : 1216 + 32 * (G.mem - 3); G.half2 = G.mem >= 3;
    if (w >= 4) loader<false>(r_x, r_w, wo, w2, b2, gw, ws, lds, G, w - 4 DIAG_PASS); else compute<false>(b1, out, ws, lds, G, w DIAG_PASS);
  }
}

}

extern "C" void kernel_launch(void* const* d_in, const int* in_sizes, int n_in, void* d_out, int out_size, void* d_ws, size_t ws_size,
                              hipStream_t stream) {
  (void)hipFuncSetAttribute((const void*)fused_kernel, hipFuncAttributeMaxDynamicSharedMemorySize, LDS_BYTES);
  fused_kernel<<<256, 512, LDS_BYTES, stream>>>(d_in[0], d_in[1], d_in[2], d_in[4], d_in[5], d_in[6], d_in[7], d_in[8], d_out, (char*)d_ws);
}
```
